```python
import torch

def _softmax(x, axis=-1):
    return torch.softmax(x, dim=axis)

def _gelu(x):
    return torch.nn.functional.gelu(x, approximate='tanh')

def _silu(x):
    return torch.nn.functional.silu(x)

def _sigmoid(x):
    return torch.sigmoid(x)

def _layernorm(x, axis=-1, eps=1e-05):
    m = x.mean(dim=axis, keepdim=True)
    v = x.var(dim=axis, keepdim=True, unbiased=False)
    return (x - m) / torch.sqrt(v + eps)

def _scatter_add(tgt, idx, src, axis=0):
    return tgt.clone().index_add_(axis, idx, src)

def _segment_sum(src, seg, n):
    out = torch.zeros((n,) + src.shape[1:], dtype=src.dtype)
    return out.index_add_(0, seg, src)

def _pad(x, pw):
    flat = tuple((v for pair in reversed(pw) for v in pair))
    return torch.nn.functional.pad(x, flat)
kernel_name = 'standalone__tree_speculative_verify_tree_attention_over_candidate_token_tree__sampling'

def setup_inputs():

    _w = lambda a: (lambda t: t.to(torch.bfloat16) if t.is_floating_point() else t)(torch.as_tensor(a))
    B, Hq, Hkv, T, C, D = (32, 32, 8, 8, 2048, 128)
    tree_mask = torch.zeros((B, T, T), dtype=float)
    for b in range(B):
        parent = torch.zeros(T, dtype=torch.int64)
        for i in range(1, T):
            parent[i] = int(torch.randint(0, i, (1,)).item())
        for i in range(T):
            j = i
            tree_mask[b, i, j] = 1.0
            while j > 0:
                j = parent[j]
                tree_mask[b, i, j] = 1.0
    return {'q': _w((lambda *s: torch.randn(*s, dtype=torch.bfloat16) * 0.8)(B, Hq, T, D)), 'k_cache': _w((lambda *s: torch.randn(*s, dtype=torch.bfloat16) * 0.8)(B, Hkv, C, D)), 'v_cache': _w((lambda *s: torch.randn(*s, dtype=torch.bfloat16) * 0.05)(B, Hkv, C, D)), 'k_tree': _w((lambda *s: torch.randn(*s, dtype=torch.bfloat16) * 0.8)(B, Hkv, T, D)), 'v_tree': _w((lambda *s: torch.randn(*s, dtype=torch.bfloat16) * 0.05)(B, Hkv, T, D)), 'tree_mask': _w(tree_mask)}

def reference(q, k_cache, v_cache, k_tree, v_tree, tree_mask):
    B, Hq, T, D = q.shape
    Hkv, C = (k_cache.shape[1], k_cache.shape[2])
    rep = Hq // Hkv
    kc = (lambda x, n, axis: torch.repeat_interleave(x, n, dim=axis))(k_cache, rep, axis=1)
    vc = (lambda x, n, axis: torch.repeat_interleave(x, n, dim=axis))(v_cache, rep, axis=1)
    kt = (lambda x, n, axis: torch.repeat_interleave(x, n, dim=axis))(k_tree, rep, axis=1)
    vt = (lambda x, n, axis: torch.repeat_interleave(x, n, dim=axis))(v_tree, rep, axis=1)
    k = torch.concatenate([kc, kt], axis=2)
    v = torch.concatenate([vc, vt], axis=2)
    scale = D ** (-0.5)
    scores = torch.einsum('bhqd,bhkd->bhqk', q, k) * scale
    ctx_mask = torch.ones((B, 1, T, C), dtype=scores.dtype)
    tm = tree_mask[:, None, :, :].clone()
    full_mask = torch.concatenate([ctx_mask, tm], axis=-1)
    scores = scores + (1.0 - full_mask) * -1000000000.0
    p = _softmax(scores, axis=-1)
    return torch.einsum('bhqk,bhkd->bhqd', p, v)
```

```cpp
#include <hip/hip_runtime.h>
#include <hip/hip_bf16.h>

typedef unsigned u32x4 __attribute__((ext_vector_type(4)));
typedef unsigned u32x2 __attribute__((ext_vector_type(2)));
typedef int i32x4 __attribute__((ext_vector_type(4)));
typedef float f32x4 __attribute__((ext_vector_type(4)));
typedef float f32x2 __attribute__((ext_vector_type(2)));
typedef short s16x4 __attribute__((ext_vector_type(4)));
typedef __bf16 bf16x8 __attribute__((ext_vector_type(8)));
typedef __bf16 bf16x2 __attribute__((ext_vector_type(2)));
#define LAS __attribute__((address_space(3)))

namespace {

constexpr int NB = 32, HKV = 8, TT = 8, CTX = 2048, HD = 128;
constexpr int NL = 4, NC = 4;
constexpr int NI = 4;
constexpr int NIT = CTX / 64;
constexpr int NSLOT = 4;
constexpr int SLOT_B = NL * 8192;
constexpr int TREE_OFF = NSLOT * SLOT_B;
constexpr int FLAG_OFF = TREE_OFF + NC * 2048;
constexpr int LDS_B = FLAG_OFF + 64;
constexpr float LOG2E = 1.4426950408889634f;
constexpr float SCL = 0.08838834764831845f * LOG2E;
constexpr int WIN = 5;
constexpr int PACE = 1850;
constexpr int LEAD = 1000;
constexpr int SPIN_CAP = 1 << 20;
#define SB() __builtin_amdgcn_sched_barrier(0)

__device__ __forceinline__ f32x4 mfma16(u32x4 a, u32x4 b, f32x4 c) {
  return __builtin_amdgcn_mfma_f32_16x16x32_bf16(__builtin_bit_cast(bf16x8, a), __builtin_bit_cast(bf16x8, b), c, 0, 0, 0);
}
__device__ __forceinline__ unsigned pack_bf16(float lo, float hi) {
  f32x2 v = {lo, hi};
  return __builtin_bit_cast(unsigned, __builtin_convertvector(v, bf16x2));
}
__device__ __forceinline__ float xg_max(float x) {
  auto s = __builtin_amdgcn_permlane16_swap(__float_as_uint(x), __float_as_uint(x), false, false);
  x = fmaxf(__uint_as_float(s[0]), __uint_as_float(s[1]));
  auto t = __builtin_amdgcn_permlane32_swap(__float_as_uint(x), __float_as_uint(x), false, false);
  return fmaxf(__uint_as_float(t[0]), __uint_as_float(t[1]));
}
__device__ __forceinline__ float xg_sum(float x) {
  auto s = __builtin_amdgcn_permlane16_swap(__float_as_uint(x), __float_as_uint(x), false, false);
  x = __uint_as_float(s[0]) + __uint_as_float(s[1]);
  auto t = __builtin_amdgcn_permlane32_swap(__float_as_uint(x), __float_as_uint(x), false, false);
  return __uint_as_float(t[0]) + __uint_as_float(t[1]);
}
__device__ __forceinline__ i32x4 mk_srd(const void* p, int bytes) {
  const unsigned long long a = (unsigned long long)p;
  return i32x4{(int)(unsigned)a, (int)(unsigned)(a >> 32) & 0xffff, bytes, 0x00020000};
}
__device__ __forceinline__ u32x4 lds_b128(unsigned a) { return *(const LAS u32x4*)(size_t)a; }
__device__ __forceinline__ u32x2 lds_tr(unsigned a) {
  return __builtin_bit_cast(u32x2, __builtin_amdgcn_ds_read_tr16_b64_v4i16((LAS s16x4*)(size_t)a));
}

#define DMA_PIECE(W, VOFF, SRD, SOFF, IMM)                                                                                 \
  asm volatile("s_waitcnt vmcnt(%3)\n\tbuffer_load_dwordx4 %0, %1, %2 offen offset:" #IMM " nt lds" ::"v"(VOFF), "s"(SRD), \
               "s"(SOFF), "i"((W) - 1)                                                                                     \
               : "memory")
#define SET_M0(X) asm volatile("s_mov_b32 m0, %0\n\ts_nop 0" ::"s"(X) : "memory")
#define PUT(VAL) asm volatile("s_mov_b64 exec, 1\n\tds_write_b32 %0, %1\n\ts_mov_b64 exec, -1" ::"v"(landed), "v"(VAL) : "memory")
#define PUBLISH(W, POS, GI) if (((W) - 1) % 4 == (POS)) PUT((GI) - ((W) - 1) / 4);
#define K_GRANULE(W, LB, SO, GI) SET_M0(LB);                                                                \
  DMA_PIECE(W, vk[0], rk, SO, 0); PUBLISH(W, 0, GI) DMA_PIECE(W, vk[1], rk, SO, 1024); PUBLISH(W, 1, GI)    \
  DMA_PIECE(W, vk[2], rk, SO, 2048); PUBLISH(W, 2, GI) DMA_PIECE(W, vk[3], rk, SO, 3072); PUBLISH(W, 3, GI)
#define V_GRANULE(W, LB, SO, GI) SET_M0(LB);                                                                \
  DMA_PIECE(W, vv[0], rv, SO, 0); PUBLISH(W, 0, GI) DMA_PIECE(W, vv[1], rv, SO, 1024); PUBLISH(W, 1, GI)    \
  DMA_PIECE(W, vv[0], rv, SO, 2048); PUBLISH(W, 2, GI) DMA_PIECE(W, vv[1], rv, SO, 3072); PUBLISH(W, 3, GI)
#define STEP(W)                                                            \
  {                                                                        \
    const int so = (w + s * NL) * 4096, so1 = so + NL * 4096;              \
    K_GRANULE(W, lw + ((s + 1) & (NSLOT - 1)) * SLOT_B, so1, 2 * s + 1)    \
    V_GRANULE(W, lw + (s & (NSLOT - 1)) * SLOT_B + 4096, so, 2 * s + 2)    \
  }

__device__ __forceinline__ void wait_min4(unsigned addr, int need) {
#pragma unroll 1
  for (int spin = 0; spin < SPIN_CAP; ++spin) {
    const i32x4 f = *(const volatile LAS i32x4*)(size_t)addr;
    const int mn = min(min(f[0], f[1]), min(f[2], f[3]));
    if (__builtin_amdgcn_readfirstlane(mn) >= need) break;
    __builtin_amdgcn_s_sleep(1);
  }
  asm volatile("" ::: "memory");
}
__device__ __forceinline__ void qk_softmax(unsigned kb, const u32x4 (&qf)[4], float& m, float& alpha, float (&p)[16]) {
  u32x4 kf[16];
#pragma unroll
  for (int t = 0; t < 4; ++t)
#pragma unroll
    for (int st = 0; st < 4; ++st) kf[t * 4 + st] = lds_b128((kb ^ (st * 64)) + t * 8192);
  SB();
  f32x4 s[4];
#pragma unroll
  for (int t = 0; t < 4; ++t) {
    s[t] = f32x4{0.f, 0.f, 0.f, 0.f};
#pragma unroll
    for (int st = 0; st < 4; ++st) s[t] = mfma16(kf[t * 4 + st], qf[st], s[t]);
  }
  float tm = fmaxf(fmaxf(s[0][0], s[0][1]), fmaxf(s[0][2], s[0][3]));
#pragma unroll
  for (int t = 1; t < 4; ++t) tm = fmaxf(tm, fmaxf(fmaxf(s[t][0], s[t][1]), fmaxf(s[t][2], s[t][3])));
  tm = xg_max(tm);
  const float mn = fmaxf(m, tm * SCL);
  alpha = __builtin_amdgcn_exp2f(m - mn);
  m = mn;
#pragma unroll
  for (int t = 0; t < 4; ++t)
#pragma unroll
    for (int r = 0; r < 4; ++r) p[t * 4 + r] = __builtin_amdgcn_exp2f(fmaf(s[t][r], SCL, -mn));
}
__device__ __forceinline__ float sum16(const float (&p)[16]) {
  return (((p[0] + p[1]) + (p[2] + p[3])) + ((p[4] + p[5]) + (p[6] + p[7]))) +
         (((p[8] + p[9]) + (p[10] + p[11])) + ((p[12] + p[13]) + (p[14] + p[15])));
}
__device__ __forceinline__ void pack_p(const float (&p)[16], u32x4 (&pf)[2]) {
#pragma unroll
  for (int h = 0; h < 2; ++h)
    pf[h] = u32x4{pack_bf16(p[8 * h], p[8 * h + 1]), pack_bf16(p[8 * h + 2], p[8 * h + 3]),
                  pack_bf16(p[8 * h + 4], p[8 * h + 5]), pack_bf16(p[8 * h + 6], p[8 * h + 7])};
}
__device__ __forceinline__ void pv(unsigned vb, const u32x4 (&pf)[2], f32x4 (&o)[NI]) {
  u32x2 vf[NI][4];
#pragma unroll
  for (int i = 0; i < NI; ++i)
#pragma unroll
    for (int t = 0; t < 4; ++t) vf[i][t] = lds_tr((vb ^ (i * 32)) + t * 8192);
  SB();
#pragma unroll
  for (int i = 0; i < NI; ++i)
#pragma unroll
    for (int h = 0; h < 2; ++h)
      o[i] = mfma16(u32x4{vf[i][2 * h][0], vf[i][2 * h][1], vf[i][2 * h + 1][0], vf[i][2 * h + 1][1]}, pf[h], o[i]);
  SB();
}

__global__ __launch_bounds__((NL + NC) * 64)
void tree_attn_kernel(const unsigned short* __restrict__ kc, const unsigned short* __restrict__ vc,
                      const unsigned short* __restrict__ q, const unsigned short* __restrict__ kt,
                      const unsigned short* __restrict__ vt, const unsigned short* __restrict__ mask,
                      unsigned short* __restrict__ out) {
  __shared__ __attribute__((aligned(256))) char smem[LDS_B];
  const unsigned lds0 = (unsigned)(size_t)smem;
  const int bh = blockIdx.x;
  const int lane = threadIdx.x & 63;
  const int w = __builtin_amdgcn_readfirstlane(threadIdx.x >> 6);
  const int g = lane >> 4, c = lane & 15;
  if (threadIdx.x < 8) *((volatile LAS int*)(size_t)(lds0 + FLAG_OFF) + threadIdx.x) = 0;

  if (w >= NL) {
    asm volatile("s_barrier" ::: "memory");
    const int cw = w - NL, u = cw & 1, dh = cw >> 1;
    volatile LAS int* done = (volatile LAS int*)(size_t)(lds0 + FLAG_OFF + 16) + cw;
    u32x4 qf[4], ktf[4], vtr[2];
    {
      const unsigned short* qp = q + ((size_t)bh * 32 + u * 16 + c) * HD + g * 8;
      const unsigned short* kp = kt + ((size_t)bh * TT + (c & 7)) * HD + g * 8;
      const unsigned short* vp = vt + (size_t)bh * TT * HD + lane * 8;
#pragma unroll
      for (int st = 0; st < 4; ++st) qf[st] = *reinterpret_cast<const u32x4*>(qp + st * 32);
#pragma unroll
      for (int st = 0; st < 4; ++st) ktf[st] = *reinterpret_cast<const u32x4*>(kp + st * 32);
#pragma unroll
      for (int n = 0; n < 2; ++n) vtr[n] = *reinterpret_cast<const u32x4*>(vp + n * 512);
    }
    const u32x2 mk = *reinterpret_cast<const u32x2*>(mask + ((size_t)(bh >> 3) * TT + (c & 7)) * TT + (g & 1) * 4);
    SB();
    const unsigned kl = c * 256 + ((g ^ c) << 4);
    const int vrow = 4 * g + (c >> 2);
    const unsigned vl = (4096 + vrow * 256 + ((vrow & 7) << 5) + (c & 3) * 8) ^ (dh * NI * 32);
    float m, l, alpha;
    f32x4 o[NI];
    {
      const unsigned tb = lds0 + TREE_OFF + cw * 2048;
#pragma unroll
      for (int n = 0; n < 2; ++n) *(LAS u32x4*)(size_t)(tb + n * 1024 + lane * 16) = vtr[n];
      f32x4 s = {0.f, 0.f, 0.f, 0.f};
#pragma unroll
      for (int st = 0; st < 4; ++st) s = mfma16(ktf[st], qf[st], s);
      float y[4];
#pragma unroll
      for (int r = 0; r < 4; ++r) {
        const float mv = __uint_as_float((r & 1) ? (mk[r >> 1] & 0xffff0000u) : (mk[r >> 1] << 16));
        const float bias = (1.0f - mv) * -1000000000.0f * LOG2E;
        y[r] = g < 2 ? fmaf(s[r], SCL, bias) : -INFINITY;
      }
      m = xg_max(fmaxf(fmaxf(y[0], y[1]), fmaxf(y[2], y[3])));
      float p[4];
#pragma unroll
      for (int r = 0; r < 4; ++r) p[r] = __builtin_amdgcn_exp2f(y[r] - m);
      l = (p[0] + p[1]) + (p[2] + p[3]);
      const u32x4 pt = {pack_bf16(p[0], p[1]), pack_bf16(p[2], p[3]), 0u, 0u};
      asm volatile("s_waitcnt lgkmcnt(0)" ::: "memory");
      const unsigned vb = tb + (vrow & 7) * 256 + (c & 3) * 8 + dh * NI * 32;
#pragma unroll
      for (int i = 0; i < NI; ++i) {
        const u32x2 a = lds_tr(vb + i * 32);
        o[i] = mfma16(u32x4{a[0], a[1], 0u, 0u}, pt, f32x4{0.f, 0.f, 0.f, 0.f});
      }
    }
    u32x4 pf[2] = {u32x4{0u, 0u, 0u, 0u}, u32x4{0u, 0u, 0u, 0u}};
    float p[16];
#pragma unroll 1
    for (int j = 0; j < NIT; ++j) {
      wait_min4(lds0 + FLAG_OFF, j == 0 ? 1 : 2 * j);
      qk_softmax(lds0 + (j & (NSLOT - 1)) * SLOT_B + kl, qf, m, alpha, p);
      l = fmaf(l, alpha, sum16(p));
      if (j == NIT - 1) {
        const float inv = 1.0f / xg_sum(l);
        alpha *= inv;
#pragma unroll
        for (int x = 0; x < 16; ++x) p[x] *= inv;
      }
      u32x4 pn[2];
      pack_p(p, pn);
      asm volatile("" : "+v"(pn[0]), "+v"(pn[1]), "+v"(l), "+v"(alpha));
      if (j > 0) {
        wait_min4(lds0 + FLAG_OFF, 2 * j + 1);
        pv(lds0 + ((j - 1) & (NSLOT - 1)) * SLOT_B + vl, pf, o);
        asm volatile("s_waitcnt lgkmcnt(0)" ::: "memory");
        if (lane == 0) *done = j;
      }
#pragma unroll
      for (int i = 0; i < NI; ++i) o[i] *= alpha;
      pf[0] = pn[0];
      pf[1] = pn[1];
    }
#pragma unroll
    for (int i = 0; i < NI; ++i) asm volatile("" : "+v"(o[i]));
    wait_min4(lds0 + FLAG_OFF, 2 * NIT);
    pv(lds0 + ((NIT - 1) & (NSLOT - 1)) * SLOT_B + vl, pf, o);
    unsigned short* op = out + ((size_t)bh * 32 + u * 16 + c) * HD + dh * NI * 16 + (g & 1) * 16 + (g >> 1) * 8;
#pragma unroll
    for (int i = 0; i < NI; i += 2) {
      const unsigned a0 = pack_bf16(o[i][0], o[i][1]), a1 = pack_bf16(o[i][2], o[i][3]);
      const unsigned b0 = pack_bf16(o[i + 1][0], o[i + 1][1]), b1 = pack_bf16(o[i + 1][2], o[i + 1][3]);
      const auto x0 = __builtin_amdgcn_permlane16_swap(a0, b0, false, false);
      const auto x1 = __builtin_amdgcn_permlane16_swap(a1, b1, false, false);
      *reinterpret_cast<u32x4*>(op + i * 16) = u32x4{x0[0], x1[0], x0[1], x1[1]};
    }
  } else {
    __builtin_amdgcn_s_setprio(3);
    const i32x4 rk = mk_srd(kc + (size_t)bh * CTX * HD, CTX * HD * 2);
    const i32x4 rv = mk_srd(vc + (size_t)bh * CTX * HD, CTX * HD * 2);
    int vk[4], vv[2];
#pragma unroll
    for (int n = 0; n < 4; ++n) vk[n] = g * 256 + ((c ^ (4 * n + g)) << 4);
#pragma unroll
    for (int n = 0; n < 2; ++n) vv[n] = g * 256 + ((c ^ ((4 * n + g) << 1)) << 4);
    const unsigned landed = lds0 + FLAG_OFF + w * 4;
    const unsigned lw = lds0 + w * 8192, done_addr = lds0 + FLAG_OFF + 16;
    SET_M0(lw);
    DMA_PIECE(WIN, vk[0], rk, w * 4096, 0); DMA_PIECE(WIN, vk[1], rk, w * 4096, 1024);
    DMA_PIECE(WIN, vk[2], rk, w * 4096, 2048); DMA_PIECE(WIN, vk[3], rk, w * 4096, 3072);
    unsigned long long tnow;
    asm volatile("s_memrealtime %0\n\ts_waitcnt lgkmcnt(0)\n\ts_barrier" : "=s"(tnow)::"memory");
    const unsigned tstart = (unsigned)tnow;
#pragma unroll 1
    for (int s = 0; s < NIT - 1; ++s) {
      i32x4 d;
      asm volatile("s_memrealtime %0\n\tds_read_b128 %1, %2\n\ts_waitcnt lgkmcnt(0)" : "=&s"(tnow), "=&v"(d) : "v"(done_addr) : "memory");
      const int need = s >= NSLOT - 1 ? max(s - (NSLOT - 1), 1) : 0;
      if (__builtin_amdgcn_readfirstlane(min(min(d[0], d[1]), min(d[2], d[3]))) < need) wait_min4(done_addr, need);
      const int late = __builtin_amdgcn_readfirstlane((int)(((unsigned)tnow - tstart) * 16u) - s * PACE);
      if (late > 0) STEP(WIN + 1) else if (late < -LEAD) STEP(WIN - 1) else STEP(WIN)
    }
    wait_min4(done_addr, NIT - NSLOT);
    V_GRANULE(WIN, lw + ((NIT - 1) & (NSLOT - 1)) * SLOT_B + 4096, (w + (NIT - 1) * NL) * 4096, 2 * NIT - 1)
    asm volatile("s_waitcnt vmcnt(4)" ::: "memory");
    PUT(2 * NIT - 1);
    asm volatile("s_waitcnt vmcnt(0)" ::: "memory");
    PUT(2 * NIT);
  }
}

}

extern "C" void kernel_launch(void* const* d_in, const int* in_sizes, int n_in, void* d_out, int out_size, void* d_ws,
                              size_t ws_size, hipStream_t stream) {
  (void)in_sizes; (void)n_in; (void)out_size; (void)d_ws; (void)ws_size;
  tree_attn_kernel<<<dim3(NB * HKV), dim3((NL + NC) * 64), 0, stream>>>(
      (const unsigned short*)d_in[1], (const unsigned short*)d_in[2], (const unsigned short*)d_in[0],
      (const unsigned short*)d_in[3], (const unsigned short*)d_in[4], (const unsigned short*)d_in[5],
      (unsigned short*)d_out);
}
```

```cpp
#include <hip/hip_runtime.h>
#include <hip/hip_bf16.h>

typedef unsigned u32x4 __attribute__((ext_vector_type(4)));
typedef unsigned u32x2 __attribute__((ext_vector_type(2)));
typedef int i32x4 __attribute__((ext_vector_type(4)));
typedef float f32x4 __attribute__((ext_vector_type(4)));
typedef float f32x2 __attribute__((ext_vector_type(2)));
typedef short s16x4 __attribute__((ext_vector_type(4)));
typedef __bf16 bf16x8 __attribute__((ext_vector_type(8)));
typedef __bf16 bf16x2 __attribute__((ext_vector_type(2)));
#define LAS __attribute__((address_space(3)))

namespace {

constexpr int NB = 32, HKV = 8, TT = 8, CTX = 2048, HD = 128;
constexpr int NL = 4, NC = 4;
constexpr int NG = CTX / 64;
constexpr int NOWN = NG / 2;
constexpr int NSLOT = 7;
constexpr int SLOT_B = NL * 4096;
constexpr int ALPHA_OFF = NSLOT * SLOT_B;
constexpr int XO_OFF = ALPHA_OFF + NC * NOWN * 256;
constexpr int XP_OFF = XO_OFF + 2 * 8192;
constexpr int XML_OFF = XP_OFF + 2 * 2048;
constexpr int FLAG_OFF = XML_OFF + NC * 512;
constexpr int LDS_B = FLAG_OFF + 64;
static_assert(LDS_B <= 160 * 1024, "LDS");
constexpr float LOG2E = 1.4426950408889634f;
constexpr float SCL = 0.08838834764831845f * LOG2E;
constexpr int WIN = 5;
constexpr int PACE = 1820;
constexpr int LEAD = 800;
constexpr int SPIN_CAP = 1 << 20;
#define SB() __builtin_amdgcn_sched_barrier(0)

__device__ __forceinline__ f32x4 mfma16(u32x4 a, u32x4 b, f32x4 c) {
  return __builtin_amdgcn_mfma_f32_16x16x32_bf16(__builtin_bit_cast(bf16x8, a), __builtin_bit_cast(bf16x8, b), c, 0, 0, 0);
}
__device__ __forceinline__ unsigned pack_bf16(float lo, float hi) {
  f32x2 v = {lo, hi};
  return __builtin_bit_cast(unsigned, __builtin_convertvector(v, bf16x2));
}
__device__ __forceinline__ float xg_max(float x) {
  auto s = __builtin_amdgcn_permlane16_swap(__float_as_uint(x), __float_as_uint(x), false, false);
  x = fmaxf(__uint_as_float(s[0]), __uint_as_float(s[1]));
  auto t = __builtin_amdgcn_permlane32_swap(__float_as_uint(x), __float_as_uint(x), false, false);
  return fmaxf(__uint_as_float(t[0]), __uint_as_float(t[1]));
}
__device__ __forceinline__ float xg_sum(float x) {
  auto s = __builtin_amdgcn_permlane16_swap(__float_as_uint(x), __float_as_uint(x), false, false);
  x = __uint_as_float(s[0]) + __uint_as_float(s[1]);
  auto t = __builtin_amdgcn_permlane32_swap(__float_as_uint(x), __float_as_uint(x), false, false);
  return __uint_as_float(t[0]) + __uint_as_float(t[1]);
}
__device__ __forceinline__ i32x4 mk_srd(const void* p, int bytes) {
  const unsigned long long a = (unsigned long long)p;
  return i32x4{(int)(unsigned)a, (int)(unsigned)(a >> 32) & 0xffff, bytes, 0x00020000};
}
__device__ __forceinline__ u32x4 lds_b128(unsigned a) { return *(const LAS u32x4*)(size_t)a; }
__device__ __forceinline__ u32x2 lds_tr(unsigned a) {
  return __builtin_bit_cast(u32x2, __builtin_amdgcn_ds_read_tr16_b64_v4i16((LAS s16x4*)(size_t)a));
}

#define DMA_PIECE(W, VOFF, SRD, SOFF, IMM)                                                                                 \
  asm volatile("s_waitcnt vmcnt(%3)\n\tbuffer_load_dwordx4 %0, %1, %2 offen offset:" #IMM " nt lds" ::"v"(VOFF), "s"(SRD), \
               "s"(SOFF), "i"((W) - 1)                                                                                     \
               : "memory")
#define SET_M0(X) asm volatile("s_mov_b32 m0, %0\n\ts_nop 0" ::"s"(X) : "memory")
#define PUT(VAL) asm volatile("s_mov_b64 exec, 1\n\tds_write_b32 %0, %1\n\ts_mov_b64 exec, -1" ::"v"(landed), "v"(VAL) : "memory")
#define PUBLISH(W, POS, GI) if (((W) - 1) % 4 == (POS)) PUT((GI) - ((W) - 1) / 4);
#define GRANULE(W, VO, RS, LB, SO, GI) SET_M0(LB);                                                          \
  DMA_PIECE(W, VO[0], RS, SO, 0); PUBLISH(W, 0, GI) DMA_PIECE(W, VO[1], RS, SO, 1024); PUBLISH(W, 1, GI)    \
  DMA_PIECE(W, VO[2], RS, SO, 2048); PUBLISH(W, 2, GI) DMA_PIECE(W, VO[3], RS, SO, 3072); PUBLISH(W, 3, GI)
#define STEP(W)                                               \
  {                                                           \
    GRANULE(W, voa, ra, lw + sl0 * SLOT_B, soa, 2 * s + 1)    \
    GRANULE(W, vob, rb, lw + sl1 * SLOT_B, sob, 2 * s + 2)    \
  }

__device__ __forceinline__ void wait_min4(unsigned addr, int need) {
#pragma unroll 1
  for (int spin = 0; spin < SPIN_CAP; ++spin) {
    const i32x4 f = *(const volatile LAS i32x4*)(size_t)addr;
    const int mn = min(min(f[0], f[1]), min(f[2], f[3]));
    if (__builtin_amdgcn_readfirstlane(mn) >= need) break;
    __builtin_amdgcn_s_sleep(1);
  }
  asm volatile("" ::: "memory");
}
__device__ __forceinline__ void wait_word(unsigned addr, int need) {
#pragma unroll 1
  for (int spin = 0; spin < SPIN_CAP; ++spin) {
    const int f = *(const volatile LAS int*)(size_t)addr;
    if (__builtin_amdgcn_readfirstlane(f) >= need) break;
    __builtin_amdgcn_s_sleep(1);
  }
  asm volatile("" ::: "memory");
}
template <int HH>
__device__ __forceinline__ void pv_half(unsigned vb, u32x4 p0, u32x4 p1, f32x4 (&o)[8]) {
  u32x2 vf[4][4];
#pragma unroll
  for (int i = 0; i < 4; ++i)
#pragma unroll
    for (int t = 0; t < 4; ++t) vf[i][t] = lds_tr((vb ^ ((HH * 4 + i) * 32)) + t * 4096);
  SB();
#pragma unroll
  for (int i = 0; i < 4; ++i) {
    o[HH * 4 + i] = mfma16(u32x4{vf[i][0][0], vf[i][0][1], vf[i][1][0], vf[i][1][1]}, p0, o[HH * 4 + i]);
    o[HH * 4 + i] = mfma16(u32x4{vf[i][2][0], vf[i][2][1], vf[i][3][0], vf[i][3][1]}, p1, o[HH * 4 + i]);
  }
  SB();
}
template <int HH>
__device__ __forceinline__ void store_half(unsigned short* op, const f32x4 (&o)[8]) {
#pragma unroll
  for (int i = HH * 4; i < HH * 4 + 4; i += 2) {
    const unsigned a0 = pack_bf16(o[i][0], o[i][1]), a1 = pack_bf16(o[i][2], o[i][3]);
    const unsigned b0 = pack_bf16(o[i + 1][0], o[i + 1][1]), b1 = pack_bf16(o[i + 1][2], o[i + 1][3]);
    const auto x0 = __builtin_amdgcn_permlane16_swap(a0, b0, false, false);
    const auto x1 = __builtin_amdgcn_permlane16_swap(a1, b1, false, false);
    *reinterpret_cast<u32x4*>(op + i * 16) = u32x4{x0[0], x1[0], x0[1], x1[1]};
  }
}

__global__ __launch_bounds__((NL + NC) * 64)
void tree_attn_kernel(const unsigned short* __restrict__ kc, const unsigned short* __restrict__ vc,
                      const unsigned short* __restrict__ q, const unsigned short* __restrict__ kt,
                      const unsigned short* __restrict__ vt, const unsigned short* __restrict__ mask,
                      unsigned short* __restrict__ out) {
  __shared__ __attribute__((aligned(256))) char smem[LDS_B];
  const unsigned lds0 = (unsigned)(size_t)smem;
  const int bh = blockIdx.x;
  const int lane = threadIdx.x & 63;
  const int w = __builtin_amdgcn_readfirstlane(threadIdx.x >> 6);
  const int g = lane >> 4, c = lane & 15;

  if (w >= NL) {
    const int cw = w - NL, u = cw & 1, kp = cw >> 1;
    if (cw == 0 && lane < 12) *((volatile LAS int*)(size_t)(lds0 + FLAG_OFF) + lane) = 0;
    u32x4 qf[4];
    {
      const unsigned short* qp = q + ((size_t)bh * 32 + u * 16 + c) * HD + g * 8;
#pragma unroll
      for (int st = 0; st < 4; ++st) qf[st] = *reinterpret_cast<const u32x4*>(qp + st * 32);
    }
    SB();
    asm volatile("s_waitcnt lgkmcnt(0)\n\ts_barrier" ::: "memory");
    volatile LAS int* done = (volatile LAS int*)(size_t)(lds0 + FLAG_OFF + 16) + cw;
    const unsigned xflag = lds0 + FLAG_OFF + 32;
    const unsigned kl = c * 256 + ((g ^ c) << 4);
    const int vrow = 4 * g + (c >> 2);
    const unsigned vl = vrow * 256 + ((vrow & 7) << 5) + (c & 3) * 8;
    const unsigned al = lds0 + ALPHA_OFF + cw * NOWN * 256 + lane * 4;
    const unsigned xo = lds0 + XO_OFF + u * 8192;
    float m = -INFINITY, l = 0.f;
    u32x2 pt = {0u, 0u};
    if (kp == 0) {
      const unsigned short* kpp = kt + ((size_t)bh * TT + (c & 7)) * HD + g * 8;
      const unsigned short* vp = vt + (size_t)bh * TT * HD + lane * 8;
      u32x4 ktf[4], vtr[2];
#pragma unroll
      for (int st = 0; st < 4; ++st) ktf[st] = *reinterpret_cast<const u32x4*>(kpp + st * 32);
#pragma unroll
      for (int n = 0; n < 2; ++n) vtr[n] = *reinterpret_cast<const u32x4*>(vp + n * 512);
      const u32x2 mk = *reinterpret_cast<const u32x2*>(mask + ((size_t)(bh >> 3) * TT + (c & 7)) * TT + (g & 1) * 4);
#pragma unroll
      for (int n = 0; n < 2; ++n) *(LAS u32x4*)(size_t)(xo + 4096 + n * 1024 + lane * 16) = vtr[n];
      f32x4 s = {0.f, 0.f, 0.f, 0.f};
#pragma unroll
      for (int st = 0; st < 4; ++st) s = mfma16(ktf[st], qf[st], s);
      float y[4];
#pragma unroll
      for (int r = 0; r < 4; ++r) {
        const float mv = __uint_as_float((r & 1) ? (mk[r >> 1] & 0xffff0000u) : (mk[r >> 1] << 16));
        const float bias = (1.0f - mv) * -1000000000.0f * LOG2E;
        y[r] = g < 2 ? fmaf(s[r], SCL, bias) : -INFINITY;
      }
      m = xg_max(fmaxf(fmaxf(y[0], y[1]), fmaxf(y[2], y[3])));
      float p[4];
#pragma unroll
      for (int r = 0; r < 4; ++r) p[r] = __builtin_amdgcn_exp2f(y[r] - m);
      l = (p[0] + p[1]) + (p[2] + p[3]);
      pt = u32x2{pack_bf16(p[0], p[1]), pack_bf16(p[2], p[3])};
    }
    u32x4 P[2 * NOWN];
#pragma unroll
    for (int x = 0; x < 2 * NOWN; ++x) P[x] = u32x4{0u, 0u, 0u, 0u};
    int slot = kp;
#pragma unroll 1
    for (int j = 0; j < NOWN; ++j) {
      const int gi = 2 * j + kp;
#pragma unroll
      for (int x = 2 * NOWN - 1; x >= 2; --x) P[x] = P[x - 2];
      wait_min4(lds0 + FLAG_OFF, gi + 1);
      const unsigned kb = lds0 + slot * SLOT_B + kl;
      f32x4 s[4];
#pragma unroll
      for (int hh = 0; hh < 2; ++hh) {
        u32x4 kf[8];
#pragma unroll
        for (int t = 0; t < 2; ++t)
#pragma unroll
          for (int st = 0; st < 4; ++st) kf[t * 4 + st] = lds_b128((kb ^ (st * 64)) + (hh * 2 + t) * 4096);
        SB();
#pragma unroll
        for (int t = 0; t < 2; ++t) {
          s[hh * 2 + t] = f32x4{0.f, 0.f, 0.f, 0.f};
#pragma unroll
          for (int st = 0; st < 4; ++st) s[hh * 2 + t] = mfma16(kf[t * 4 + st], qf[st], s[hh * 2 + t]);
        }
        SB();
      }
      asm volatile("s_waitcnt lgkmcnt(0)" ::: "memory");
      if (lane == 0) *done = gi + 2;
      float tm = fmaxf(fmaxf(s[0][0], s[0][1]), fmaxf(s[0][2], s[0][3]));
#pragma unroll
      for (int t = 1; t < 4; ++t) tm = fmaxf(tm, fmaxf(fmaxf(s[t][0], s[t][1]), fmaxf(s[t][2], s[t][3])));
      tm = xg_max(tm);
      const float mn = fmaxf(m, tm * SCL);
      float alpha = __builtin_amdgcn_exp2f(m - mn);
      m = mn;
      float p[16];
#pragma unroll
      for (int t = 0; t < 4; ++t)
#pragma unroll
        for (int r = 0; r < 4; ++r) p[t * 4 + r] = __builtin_amdgcn_exp2f(fmaf(s[t][r], SCL, -mn));
      l = fmaf(l, alpha, (((p[0] + p[1]) + (p[2] + p[3])) + ((p[4] + p[5]) + (p[6] + p[7]))) +
                             (((p[8] + p[9]) + (p[10] + p[11])) + ((p[12] + p[13]) + (p[14] + p[15]))));
      if (j == NOWN - 1) {
        const float ls = xg_sum(l);
        *(LAS f32x2*)(size_t)(lds0 + XML_OFF + cw * 512 + lane * 8) = f32x2{m, ls};
        asm volatile("s_waitcnt lgkmcnt(0)" ::: "memory");
        if (lane == 0) *(volatile LAS int*)(size_t)(xflag + cw * 4) = 1;
        wait_word(xflag + (cw ^ 2) * 4, 1);
        const f32x2 ot = *(const volatile LAS f32x2*)(size_t)(lds0 + XML_OFF + (cw ^ 2) * 512 + lane * 8);
        const float M = fmaxf(m, ot[0]);
        const float e0 = __builtin_amdgcn_exp2f(m - M), e1 = __builtin_amdgcn_exp2f(ot[0] - M);
        const float fin = e0 / fmaf(ls, e0, ot[1] * e1);
        alpha *= fin;
#pragma unroll
        for (int x = 0; x < 16; ++x) p[x] *= fin;
      }
      *(LAS float*)(size_t)(al + j * 256) = alpha;
      P[0] = u32x4{pack_bf16(p[0], p[1]), pack_bf16(p[2], p[3]), pack_bf16(p[4], p[5]), pack_bf16(p[6], p[7])};
      P[1] = u32x4{pack_bf16(p[8], p[9]), pack_bf16(p[10], p[11]), pack_bf16(p[12], p[13]), pack_bf16(p[14], p[15])};
      slot += 2;
      if (slot >= NSLOT) slot -= NSLOT;
    }
    const unsigned xp = lds0 + XP_OFF + u * 2048 + lane * 16;
    if (kp == 1) {
      *(LAS u32x4*)(size_t)xp = P[0];
      *(LAS u32x4*)(size_t)(xp + 1024) = P[1];
    }
    f32x4 o[8];
    {
      const unsigned vb = xo + 4096 + (vrow & 7) * 256 + (c & 3) * 8;
#pragma unroll
      for (int i = 0; i < 8; ++i) {
        o[i] = f32x4{0.f, 0.f, 0.f, 0.f};
        if (kp == 0) {
          const u32x2 a = lds_tr(vb + i * 32);
          o[i] = mfma16(u32x4{a[0], a[1], 0u, 0u}, u32x4{pt[0], pt[1], 0u, 0u}, o[i]);
        }
      }
    }
#pragma unroll 1
    for (int j = 0; j < NOWN; ++j) {
      const int gi = NG + 2 * j + kp;
      const float alpha = *(const LAS float*)(size_t)(al + j * 256);
#pragma unroll
      for (int i = 0; i < 8; ++i) o[i] *= alpha;
      if (gi == 2 * NG - 1) break;
#pragma unroll
      for (int i = 0; i < 8; ++i) asm volatile("" : "+v"(o[i]));
      wait_min4(lds0 + FLAG_OFF, gi + 1);
      pv_half<1>(lds0 + slot * SLOT_B + vl, P[2 * NOWN - 2], P[2 * NOWN - 1], o);
      pv_half<0>(lds0 + slot * SLOT_B + vl, P[2 * NOWN - 2], P[2 * NOWN - 1], o);
      asm volatile("s_waitcnt lgkmcnt(0)" ::: "memory");
      if (lane == 0) *done = gi + 2;
      if (j < NOWN - 1) {
#pragma unroll
        for (int x = 2 * NOWN - 1; x >= 2; --x) P[x] = P[x - 2];
      }
      slot += 2;
      if (slot >= NSLOT) slot -= NSLOT;
    }
    const unsigned vlast = lds0 + ((2 * NG - 1) % NSLOT) * SLOT_B + vl;
    unsigned short* op = out + ((size_t)bh * 32 + u * 16 + c) * HD + (g & 1) * 16 + (g >> 1) * 8;
    if (kp == 0) {
#pragma unroll
      for (int i = 4; i < 8; ++i) *(LAS f32x4*)(size_t)(xo + i * 1024 + lane * 16) = o[i];
      asm volatile("s_waitcnt lgkmcnt(0)" ::: "memory");
      if (lane == 0) *(volatile LAS int*)(size_t)(xflag + cw * 4) = 2;
      wait_word(xflag + (cw ^ 2) * 4, 2);
      u32x4 p0 = lds_b128(xp), p1 = lds_b128(xp + 1024);
#pragma unroll
      for (int i = 0; i < 4; ++i) o[i] += *(const LAS f32x4*)(size_t)(xo + i * 1024 + lane * 16);
      asm volatile("" : "+v"(o[0]), "+v"(o[1]), "+v"(o[2]), "+v"(o[3]), "+v"(p0), "+v"(p1));
      wait_min4(lds0 + FLAG_OFF, 2 * NG);
      pv_half<0>(vlast, p0, p1, o);
      store_half<0>(op, o);
    } else {
#pragma unroll
      for (int i = 0; i < 4; ++i) *(LAS f32x4*)(size_t)(xo + i * 1024 + lane * 16) = o[i];
      asm volatile("s_waitcnt lgkmcnt(0)" ::: "memory");
      if (lane == 0) *(volatile LAS int*)(size_t)(xflag + cw * 4) = 2;
      wait_word(xflag + (cw ^ 2) * 4, 2);
#pragma unroll
      for (int i = 4; i < 8; ++i) o[i] += *(const LAS f32x4*)(size_t)(xo + i * 1024 + lane * 16);
      asm volatile("" : "+v"(o[4]), "+v"(o[5]), "+v"(o[6]), "+v"(o[7]));
      wait_min4(lds0 + FLAG_OFF, 2 * NG);
      pv_half<1>(vlast, P[2 * NOWN - 2], P[2 * NOWN - 1], o);
      store_half<1>(op, o);
    }
  } else {
    __builtin_amdgcn_s_setprio(3);
    i32x4 ra = mk_srd(kc + (size_t)bh * CTX * HD, CTX * HD * 2);
    int voa[4], vob[4];
#pragma unroll
    for (int n = 0; n < 4; ++n) voa[n] = g * 256 + ((c ^ (4 * n + g)) << 4);
    const unsigned landed = lds0 + FLAG_OFF + w * 4;
    const unsigned lw = lds0 + w * 4096, done_addr = lds0 + FLAG_OFF + 16;
    SET_M0(lw);
    DMA_PIECE(WIN, voa[0], ra, w * 4096, 0); DMA_PIECE(WIN, voa[1], ra, w * 4096, 1024);
    DMA_PIECE(WIN, voa[2], ra, w * 4096, 2048); DMA_PIECE(WIN, voa[3], ra, w * 4096, 3072);
    unsigned long long tnow;
    asm volatile("s_memrealtime %0\n\ts_waitcnt lgkmcnt(0)\n\ts_barrier" : "=s"(tnow)::"memory");
    const unsigned tstart = (unsigned)tnow;
    i32x4 rb = ra;
#pragma unroll
    for (int n = 0; n < 4; ++n) vob[n] = voa[n];
    int sl0 = 1;
#pragma unroll 1
    for (int s = 0; s < NG - 1; ++s) {
      i32x4 d;
      asm volatile("s_memrealtime %0\n\tds_read_b128 %1, %2\n\ts_waitcnt lgkmcnt(0)" : "=&s"(tnow), "=&v"(d) : "v"(done_addr) : "memory");
      const int need = 2 * s + 2 - NSLOT + 1;
      if (__builtin_amdgcn_readfirstlane(min(min(d[0], d[1]), min(d[2], d[3]))) < need) wait_min4(done_addr, need);
      const int late = __builtin_amdgcn_readfirstlane((int)(((unsigned)tnow - tstart) * 16u) - s * PACE);
      if (s == NG / 2) {
        ra = rb;
#pragma unroll
        for (int n = 0; n < 4; ++n) voa[n] = vob[n];
      }
      if (s == NG / 2 - 1) {
        rb = mk_srd(vc + (size_t)bh * CTX * HD, CTX * HD * 2);
#pragma unroll
        for (int n = 0; n < 4; ++n) vob[n] = g * 256 + ((c ^ (((4 * n + g) & 7) << 1)) << 4);
      }
      const int soa = (w + ((2 * s + 1) & (NG - 1)) * NL) * 4096, sob = (w + ((2 * s + 2) & (NG - 1)) * NL) * 4096;
      const int sl1 = sl0 + 1 >= NSLOT ? sl0 + 1 - NSLOT : sl0 + 1;
      if (late > 0) STEP(WIN + 1) else if (late < -LEAD) STEP(WIN - 1) else STEP(WIN)
      sl0 = sl1 + 1 >= NSLOT ? sl1 + 1 - NSLOT : sl1 + 1;
    }
    wait_min4(done_addr, 2 * NG - NSLOT);
    GRANULE(WIN, vob, rb, lw + sl0 * SLOT_B, (w + (NG - 1) * NL) * 4096, 2 * NG - 1)
    asm volatile("s_waitcnt vmcnt(4)" ::: "memory");
    PUT(2 * NG - 1);
    asm volatile("s_waitcnt vmcnt(0)" ::: "memory");
    PUT(2 * NG);
  }
}

}

extern "C" void kernel_launch(void* const* d_in, const int* in_sizes, int n_in, void* d_out, int out_size, void* d_ws,
                              size_t ws_size, hipStream_t stream) {
  (void)in_sizes; (void)n_in; (void)out_size; (void)d_ws; (void)ws_size;
  tree_attn_kernel<<<dim3(NB * HKV), dim3((NL + NC) * 64), 0, stream>>>(
      (const unsigned short*)d_in[1], (const unsigned short*)d_in[2], (const unsigned short*)d_in[0],
      (const unsigned short*)d_in[3], (const unsigned short*)d_in[4], (const unsigned short*)d_in[5],
      (unsigned short*)d_out);
}
```
